```python
import torch

def _softmax(x, axis=-1):
    return torch.softmax(x, dim=axis)
def _gelu(x):
    return torch.nn.functional.gelu(x, approximate="tanh")
def _silu(x):
    return torch.nn.functional.silu(x)
def _sigmoid(x):
    return torch.sigmoid(x)
def _layernorm(x, axis=-1, eps=1e-5):
    m = x.mean(dim=axis, keepdim=True)
    v = x.var(dim=axis, keepdim=True, unbiased=False)
    return (x - m) / torch.sqrt(v + eps)
def _scatter_add(tgt, idx, src, axis=0):
    return tgt.clone().index_add_(axis, idx, src)
def _segment_sum(src, seg, n):
    out = torch.zeros((n,) + src.shape[1:], dtype=src.dtype)
    return out.index_add_(0, seg, src)
def _pad(x, pw):
    flat = tuple(v for pair in reversed(pw) for v in pair)
    return torch.nn.functional.pad(x, flat)

kernel_name = 'standalone__mha_block_cached__near_ridge'

def setup_inputs():

    _w = (lambda a: (lambda t: t.to(torch.bfloat16) if t.is_floating_point() else t)(torch.as_tensor(a)))
    B, Sq, D = 16, 144, 448
    H, Sk, Dh = 16, 512, 256
    return {
        "x": _w((lambda *s: torch.randn(*s, dtype=torch.bfloat16) * 0.8)(B, Sq, D)),
        "k_cache": _w((lambda *s: torch.randn(*s, dtype=torch.bfloat16) * 0.8)(B, H, Sk, Dh)),
        "v_cache": _w((lambda *s: torch.randn(*s, dtype=torch.bfloat16) * 0.05)(B, H, Sk, Dh)),
        "wq": _w((lambda *s: torch.randn(*s, dtype=torch.bfloat16) * 0.8)(D, H * Dh) * (D ** -0.5)),
        "wo": _w((lambda *s: torch.randn(*s, dtype=torch.bfloat16) * 0.05)(H * Dh, D) * ((H * Dh) ** -0.5)),
    }

def reference(x, k_cache, v_cache, wq, wo):
    B, Sq, D = x.shape
    H, Dh = 16, 256
    q = (lambda x, *d: x.permute(*d))((x @ wq).reshape(B, Sq, H, Dh), 0, 2, 1, 3)
    scores = torch.einsum("bhqd,bhkd->bhqk", q, k_cache) * (Dh ** -0.5)
    p = _softmax(scores, axis=-1)
    o = torch.einsum("bhqk,bhkd->bhqd", p, v_cache)
    return (lambda x, *d: x.permute(*d))(o, 0, 2, 1, 3).reshape(B, Sq, H * Dh) @ wo
```

```cpp
#include <hip/hip_runtime.h>
#include <hip/hip_bf16.h>

typedef __attribute__((ext_vector_type(8))) short bf16x8;
typedef __attribute__((ext_vector_type(4))) short s16x4;
typedef __attribute__((ext_vector_type(4))) float f32x4;
typedef __attribute__((ext_vector_type(4))) unsigned u32x4;
typedef __attribute__((ext_vector_type(2))) unsigned u32x2;
typedef __attribute__((ext_vector_type(2))) __bf16 bf16x2;
#define AS3 __attribute__((address_space(3)))
#define DI __device__ __forceinline__
typedef AS3 char* lptr;

constexpr int SQ = 144, DM = 448, NH = 16, SK = 512, DH = 256;
constexpr int ST_BYTES = 51200;
constexpr int ST_X = 0, ST_W = 18432;
constexpr int SLOT = 16384;
constexpr int ZR = 81920;
constexpr int ZROW = 528;
constexpr int ZT = 16 * ZROW;
constexpr int PBUF = 18432;
constexpr int SLOT56 = ZR + 2 * PBUF;
constexpr int SMALL = ZR + SQ * ZROW;
constexpr int MAXA1 = SMALL;
constexpr int MAXA2 = SMALL + 576;
constexpr int SUMA = SMALL + 1152;
constexpr int LDS_TOTAL = 163840;
static_assert(SUMA + 4608 <= LDS_TOTAL, "lds");
constexpr int PART_WG = 131072;

DI constexpr int vslot_off(int s) { return (s % 7) < 5 ? (s % 7) * SLOT : SLOT56 + ((s % 7) - 5) * SLOT; }
DI constexpr int wslot_off(int t) { return ((t + 2) % 5) * SLOT; }

#define MFMA(a, b, c) __builtin_amdgcn_mfma_f32_16x16x32_bf16((a), (b), (c), 0, 0, 0)
#define WAITV(n) asm volatile("s_waitcnt vmcnt(%0)" ::"n"(n) : "memory")
#define BARRIER() asm volatile("s_waitcnt lgkmcnt(0)\n\ts_barrier" ::: "memory")
#define SBAR() __builtin_amdgcn_sched_barrier(0)
#define OPAQUE(x) asm volatile("" : "+v"(x))

DI bf16x8 ld128(lptr p) { return *(AS3 const bf16x8*)p; }
DI bf16x8 ldtr(lptr p0, lptr p1) {
    s16x4 a = __builtin_amdgcn_ds_read_tr16_b64_v4i16((AS3 s16x4*)p0);
    s16x4 b = __builtin_amdgcn_ds_read_tr16_b64_v4i16((AS3 s16x4*)p1);
    return __builtin_shufflevector(a, b, 0, 1, 2, 3, 4, 5, 6, 7);
}
DI unsigned pk2(float a, float b) {
    bf16x2 v = {(__bf16)a, (__bf16)b};
    return __builtin_bit_cast(unsigned, v);
}
DI void dma16(__amdgpu_buffer_rsrc_t rs, lptr dst, int voff, int soff, const int aux) {
    const unsigned d = (unsigned)(size_t)dst;
    if (aux == 2)
        asm volatile("s_mov_b32 m0, %0\n\ts_nop 0\n\tbuffer_load_dwordx4 %1, %2, %3 offen nt lds" ::"s"(d), "v"(voff), "s"(rs), "s"(soff) : "m0", "memory");
    else
        asm volatile("s_mov_b32 m0, %0\n\ts_nop 0\n\tbuffer_load_dwordx4 %1, %2, %3 offen lds" ::"s"(d), "v"(voff), "s"(rs), "s"(soff) : "m0", "memory");
}

constexpr int cnt_of(int grp, int id) { return id < 5 ? (grp == 1 ? 4 : 0) : 2; }
constexpr int outstanding_after(int grp, int need, int last) {
    int n = 0;
    for (int i = need + 1; i <= last; ++i) n += cnt_of(grp, i);
    return n;
}

template <int GRP>
DI void wave_main(lptr smem, const int wq, const int lane_in,
                  __amdgpu_buffer_rsrc_t rsX, __amdgpu_buffer_rsrc_t rsWq, __amdgpu_buffer_rsrc_t rsK,
                  __amdgpu_buffer_rsrc_t rsV, __amdgpu_buffer_rsrc_t rsWo, __amdgpu_buffer_rsrc_t rsP) {
    constexpr int NQ1 = GRP == 0 ? 5 : 4, Q01 = GRP == 0 ? 0 : 5;
    constexpr int NQ3 = GRP == 0 ? 3 : 6, Q03 = GRP == 0 ? 0 : 3;
    constexpr int NQ4 = GRP == 0 ? 5 : 4, Q04 = GRP == 0 ? 0 : 5;
    constexpr float CS = 0.0625f * 1.4426950408889634f;
    const int w = GRP * 4 + wq;

    bf16x8 kf[4][8];
#define KLOAD(jt, s) kf[jt][s] = __builtin_bit_cast(bf16x8, __builtin_amdgcn_raw_buffer_load_b128(rsK, kvoff + 64 * (s), (jt) * 32768, 2))

    {
        int lane = lane_in;
        const int l15 = lane & 15, g = lane >> 4, hi = lane >> 5, qq = l15 >> 2, p = lane & 3;
        const int kvoff = ((GRP == 0 ? 256 : 0) + 16 * wq + l15) * 512 + 16 * g;
        const int fV = (qq << 2) | ((g & 1) << 1);
        int aoff[4];
#pragma unroll
        for (int dt = 0; dt < 4; ++dt) aoff[dt] = (8 * g + qq) * 512 + (((8 * wq + 2 * dt + (p >> 1)) ^ fV) << 4) + 8 * (p & 1);
        const int fX = l15 >> 1;
        int boff[2];
#pragma unroll
        for (int ks = 0; ks < 2; ++ks) boff[ks] = (Q01 * 16 + l15) * 128 + (((4 * ks + g) ^ fX) << 4);

        f32x4 acc1[4][NQ1];
#pragma unroll
        for (int dt = 0; dt < 4; ++dt)
#pragma unroll
            for (int j = 0; j < NQ1; ++j) acc1[dt][j] = f32x4{0, 0, 0, 0};

        const int rW = 2 * wq + hi;
        const int wv0 = rW * 8192 + (((lane & 31) ^ ((rW & 3) << 2)) << 4);
        const int wv1 = rW * 8192 + (((lane & 31) ^ (((rW & 3) << 2) | 2)) << 4);
        const int xv = (lane >> 3) * 896 + (((lane & 7) ^ (((lane >> 4) + 4 * (wq & 1)) & 7)) << 4);
        auto stage_issue = [&](int i) {
            lptr st = smem + (i % 3) * ST_BYTES;
#pragma unroll
            for (int j = 0; j < 8; ++j)
                dma16(rsWq, st + ST_W + (wq + 4 * j) * 1024, (j & 1) ? wv1 : wv0, (64 * i + 8 * j) * 8192, 0);
#pragma unroll
            for (int j = 0; j < 5; ++j) {
                int n = wq + 4 * j;
                if (j == 4) n = 16 + (wq & 1);
                dma16(rsX, st + ST_X + n * 1024, xv, n * 8 * 896 + 128 * i, 0);
            }
        };
        if (GRP == 0) {
            stage_issue(0); stage_issue(1); stage_issue(2);
        } else {
#pragma unroll
            for (int s = 0; s < 8; ++s) KLOAD(0, s);
            if (wq == 0) {
                if (lane < 36) {
                    *(AS3 f32x4*)(smem + MAXA1 + lane * 16) = f32x4{-INFINITY, -INFINITY, -INFINITY, -INFINITY};
                    *(AS3 f32x4*)(smem + MAXA2 + lane * 16) = f32x4{-INFINITY, -INFINITY, -INFINITY, -INFINITY};
                }
            }
        }
#pragma unroll
        for (int i = 0; i < 7; ++i) {
            if (GRP == 0) {
                if (i == 0) WAITV(26); else if (i < 6) WAITV(13); else WAITV(0);
            }
            BARRIER();
            if (GRP == 0) {
                if (i >= 1 && i + 2 <= 6) stage_issue(i + 2);
                if (i == 6) {
#pragma unroll
                    for (int s = 0; s < 8; ++s) KLOAD(0, s);
#pragma unroll
                    for (int s = 0; s < 8; ++s) KLOAD(1, s);
                }
            } else if (i >= 1) {
#pragma unroll
                for (int u = 0; u < 4; ++u) { const int id = 8 + (i - 1) * 4 + u; KLOAD(id >> 3, id & 7); }
            }
            lptr st = smem + (i % 3) * ST_BYTES;
#pragma unroll
            for (int ks = 0; ks < 2; ++ks) {
                bf16x8 a[4], b[NQ1];
#pragma unroll
                for (int dt = 0; dt < 4; ++dt) a[dt] = ldtr(st + ST_W + aoff[dt] + ks * 16384, st + ST_W + aoff[dt] + ks * 16384 + 2048);
#pragma unroll
                for (int j = 0; j < NQ1; ++j) b[j] = ld128(st + ST_X + boff[ks] + j * 2048);
#pragma unroll
                for (int dt = 0; dt < 4; ++dt)
#pragma unroll
                    for (int j = 0; j < NQ1; ++j) acc1[dt][j] = MFMA(a[dt], b[j], acc1[dt][j]);
            }
        }
        int zw = ZR + (Q01 * 16 + l15) * ZROW + 256 * (g >> 1) + 32 * wq + 8 * (g & 1); OPAQUE(zw);
#pragma unroll
        for (int dt = 0; dt < 4; ++dt)
#pragma unroll
            for (int j = 0; j < NQ1; ++j) {
                u32x2 v = {pk2(acc1[dt][j][0], acc1[dt][j][1]), pk2(acc1[dt][j][2], acc1[dt][j][3])};
                *(AS3 u32x2*)(smem + zw + 128 * (dt & 1) + 16 * (dt >> 1) + j * ZT) = v;
            }
        if (GRP == 0) {
#pragma unroll
            for (int s = 0; s < 8; ++s) KLOAD(2, s);
#pragma unroll
            for (int s = 0; s < 8; ++s) KLOAD(3, s);
        }
        BARRIER();
    }

    f32x4 S[4][9];
    unsigned Pk[4][9][2];
    float lsum[9];
    {
        int lane = lane_in; OPAQUE(lane);
        const int l15 = lane & 15, g = lane >> 4, hi = lane >> 5;
        int zr = ZR + l15 * ZROW + 256 * (g & 1) + 128 * (g >> 1); OPAQUE(zr);
        int sm = SMALL + l15 * 4; OPAQUE(sm);
        int sms = SUMA + l15 * 32 + w * 4; OPAQUE(sms);
        const int rVy = 2 * wq + hi;
        const int vy0 = rVy * 512 + (((lane & 31) ^ ((rVy & 3) << 2)) << 4);
        const int vy1 = vy0 ^ 32;
        auto v_issue_y = [&](int s) {
#pragma unroll
            for (int j = 0; j < 4; ++j)
                dma16(rsV, smem + vslot_off(s) + (wq + 4 * j) * 1024, (j & 1) ? vy1 : vy0, s * 16384 + j * 4096, 2);
        };
#pragma unroll
        for (int jt = 0; jt < 4; ++jt)
#pragma unroll
            for (int j = 0; j < 9; ++j) S[jt][j] = f32x4{0, 0, 0, 0};
#pragma unroll
        for (int hf = 0; hf < 2; ++hf) {
            bf16x8 bc[3], bn[3];
#pragma unroll
            for (int u = 0; u < 3; ++u) bc[u] = ld128(smem + zr + u * ZT);
#pragma unroll
            for (int st = 0; st < 24; ++st) {
                const int s = st / 3, jb = st % 3;
                if (st + 1 < 24) {
                    const int s1 = (st + 1) / 3, jb1 = (st + 1) % 3;
#pragma unroll
                    for (int u = 0; u < 3; ++u) bn[u] = ld128(smem + zr + 16 * s1 + (3 * jb1 + u) * ZT);
                }
#pragma unroll
                for (int jt = 0; jt < 2; ++jt)
#pragma unroll
                    for (int u = 0; u < 3; ++u)
                        S[2 * hf + jt][3 * jb + u] = MFMA(kf[2 * hf + jt][s], bc[u], S[2 * hf + jt][3 * jb + u]);
#pragma unroll
                for (int u = 0; u < 3; ++u) bc[u] = bn[u];
                SBAR();
            }
            if (GRP == 1) {
                if (hf == 0) {
#pragma unroll
                    for (int s = 0; s < 8; ++s) { OPAQUE(kf[2][s]); OPAQUE(kf[3][s]); }
                    v_issue_y(0); v_issue_y(1);
                } else {
                    v_issue_y(2); v_issue_y(3); v_issue_y(4);
                }
            } else if (hf == 0) {
                BARRIER();
            }
            SBAR();
        }
#pragma unroll
        for (int j = 0; j < 9; ++j) {
            float m = S[0][j][0];
#pragma unroll
            for (int jt = 0; jt < 4; ++jt)
#pragma unroll
                for (int r = 0; r < 4; ++r) m = fmaxf(m, S[jt][j][r]);
            asm volatile("ds_max_f32 %0, %1 offset:%2" ::"v"((unsigned)(size_t)(smem + sm)), "v"(m), "n"((GRP == 1 ? 0 : 576) + 64 * j) : "memory");
        }
        if (GRP == 1) {
            BARRIER();
#pragma unroll
            for (int j = 0; j < 9; ++j) {
                const float mk = *(AS3 const float*)(smem + sm + 64 * j) * CS;
                float ls = 0.f;
#pragma unroll
                for (int jt = 0; jt < 4; ++jt) {
                    float e0 = __builtin_amdgcn_exp2f(fmaf(S[jt][j][0], CS, -mk));
                    float e1 = __builtin_amdgcn_exp2f(fmaf(S[jt][j][1], CS, -mk));
                    float e2 = __builtin_amdgcn_exp2f(fmaf(S[jt][j][2], CS, -mk));
                    float e3 = __builtin_amdgcn_exp2f(fmaf(S[jt][j][3], CS, -mk));
                    ls += (e0 + e1) + (e2 + e3);
                    Pk[jt][j][0] = pk2(e0, e1); Pk[jt][j][1] = pk2(e2, e3);
                    OPAQUE(Pk[jt][j][0]); OPAQUE(Pk[jt][j][1]);
                }
                ls += __shfl_xor(ls, 16);
                ls += __shfl_xor(ls, 32);
                if (g == 0) *(AS3 float*)(smem + sms + 512 * j) = ls;
            }
        }
        BARRIER();
    }

    f32x4 acc3[4][NQ3];
    {
        int lane = lane_in; OPAQUE(lane);
        const int l15 = lane & 15, g = lane >> 4, hi = lane >> 5, qq = l15 >> 2, p = lane & 3;
        const int fV = (qq << 2) | ((g & 1) << 1);
        int aoff[4];
#pragma unroll
        for (int dt = 0; dt < 4; ++dt) aoff[dt] = (8 * g + qq) * 512 + (((8 * wq + 2 * dt + (p >> 1)) ^ fV) << 4) + 8 * (p & 1);
        const int fX = l15 >> 1;
        int boff[2];
#pragma unroll
        for (int ks = 0; ks < 2; ++ks) { boff[ks] = ZR + (Q03 * 16 + l15) * 128 + (((4 * ks + g) ^ fX) << 4); OPAQUE(boff[ks]); }
        const int rVa = 2 * w + hi;
        const int va = rVa * 512 + (((lane & 31) ^ (((rVa & 3) << 2) | (((rVa >> 3) & 1) << 1))) << 4);
        auto v_issue_all = [&](int s) {
#pragma unroll
            for (int j = 0; j < 2; ++j)
                dma16(rsV, smem + vslot_off(s) + (w + 8 * j) * 1024, va, s * 16384 + j * 8192, 2);
        };
        int wov[2];
#pragma unroll
        for (int j = 0; j < 2; ++j) {
            int n = w + 8 * j; if (n >= 14) n = w;
            int idx = n * 64 + lane, rr = idx / 56, sl = idx % 56;
            int fW = ((rr >> 1) & 1) | (((rr >> 3) & 1) << 1);
            wov[j] = rr * 896 + ((sl ^ (fW << 1)) << 4);
        }
        auto wo_issue = [&](int t) {
#pragma unroll
            for (int j = 0; j < 2; ++j) {
                int n = w + 8 * j; if (n >= 14) n = w;
                dma16(rsWo, smem + wslot_off(t) + n * 1024, wov[j], t * 14336, 0);
            }
        };
        auto seq_issue = [&](int id) { if (id < 16) v_issue_all(id); else if (id < 32) wo_issue(id - 16); };
        int pwoff = ZR + l15 * 128 + (((2 * wq + (g >> 1)) ^ fX) << 4) + 8 * (g & 1); OPAQUE(pwoff);
        auto pwrite = [&](int i) {
#pragma unroll
            for (int j = 0; j < 9; ++j) {
                u32x2 v = {Pk[i & 3][j][0], Pk[i & 3][j][1]};
                *(AS3 u32x2*)(smem + (i & 1) * PBUF + pwoff + j * 2048) = v;
            }
        };
        int sm = SMALL + l15 * 4; OPAQUE(sm);
        int sms = SUMA + l15 * 32; OPAQUE(sms);
        float mk[9];
        auto xexp = [&](int jt) {
#pragma unroll
            for (int j = 0; j < 9; ++j) {
                if (jt == 0) {
                    const float m1 = *(AS3 const float*)(smem + sm + 64 * j), m2 = *(AS3 const float*)(smem + sm + 576 + 64 * j);
                    mk[j] = fmaxf(m1, m2) * CS;
                }
                float e0 = __builtin_amdgcn_exp2f(fmaf(S[jt][j][0], CS, -mk[j]));
                float e1 = __builtin_amdgcn_exp2f(fmaf(S[jt][j][1], CS, -mk[j]));
                float e2 = __builtin_amdgcn_exp2f(fmaf(S[jt][j][2], CS, -mk[j]));
                float e3 = __builtin_amdgcn_exp2f(fmaf(S[jt][j][3], CS, -mk[j]));
                const float ls = (e0 + e1) + (e2 + e3);
                lsum[j] = jt == 0 ? ls : lsum[j] + ls;
                Pk[jt][j][0] = pk2(e0, e1); Pk[jt][j][1] = pk2(e2, e3);
                OPAQUE(Pk[jt][j][0]); OPAQUE(Pk[jt][j][1]); OPAQUE(lsum[j]);
                if (jt == 3) {
                    float s = lsum[j];
                    s += __shfl_xor(s, 16);
                    s += __shfl_xor(s, 32);
                    if (g == 0) *(AS3 float*)(smem + sms + w * 4 + 512 * j) = s;
                }
            }
        };
        v_issue_all(5); v_issue_all(6);
        if (GRP == 1) pwrite(0); else xexp(0);
#pragma unroll
        for (int dt = 0; dt < 4; ++dt)
#pragma unroll
            for (int j = 0; j < NQ3; ++j) acc3[dt][j] = f32x4{0, 0, 0, 0};
#define LAST_BEFORE(i) ((i) <= 1 ? 6 : ((i) <= 7 ? 2 * (i) + 4 : 18))
#pragma unroll
        for (int i = 0; i < 8; ++i) {
            WAITV(outstanding_after(GRP, 2 * i + 1, LAST_BEFORE(i)));
            BARRIER();
            if (i >= 1 && i <= 6) { seq_issue(2 * i + 5); seq_issue(2 * i + 6); }
            if (i + 1 < 8 && ((i + 1 < 4) == (GRP == 1))) pwrite(i + 1);
            if (GRP == 0 && i < 3) xexp(i + 1);
            if (i == 4) {
#pragma unroll
                for (int j = 0; j < NQ3; ++j) {
                    const float m1 = *(AS3 const float*)(smem + sm + 64 * (Q03 + j)), m2 = *(AS3 const float*)(smem + sm + 576 + 64 * (Q03 + j));
                    const float f = __builtin_amdgcn_exp2f((m1 - fmaxf(m1, m2)) * CS);
#pragma unroll
                    for (int dt = 0; dt < 4; ++dt) acc3[dt][j] *= f;
                }
            }
#pragma unroll
            for (int ks = 0; ks < 2; ++ks) {
                lptr vs = smem + vslot_off(2 * i + ks);
                bf16x8 a[4], b[NQ3];
#pragma unroll
                for (int dt = 0; dt < 4; ++dt) a[dt] = ldtr(vs + aoff[dt], vs + aoff[dt] + 2048);
#pragma unroll
                for (int j = 0; j < NQ3; ++j) b[j] = ld128(smem + (i & 1) * PBUF + boff[ks] + j * 2048);
#pragma unroll
                for (int dt = 0; dt < 4; ++dt)
#pragma unroll
                    for (int j = 0; j < NQ3; ++j) acc3[dt][j] = MFMA(a[dt], b[j], acc3[dt][j]);
            }
        }
        float rl[NQ3];
#pragma unroll
        for (int j = 0; j < NQ3; ++j) {
            const float m1 = *(AS3 const float*)(smem + sm + 64 * (Q03 + j)), m2 = *(AS3 const float*)(smem + sm + 576 + 64 * (Q03 + j));
            const float f = __builtin_amdgcn_exp2f((m1 - fmaxf(m1, m2)) * CS);
            f32x4 s0 = *(AS3 const f32x4*)(smem + sms + (Q03 + j) * 512);
            f32x4 s1 = *(AS3 const f32x4*)(smem + sms + (Q03 + j) * 512 + 16);
            rl[j] = 1.0f / (((s0[0] + s0[1]) + (s0[2] + s0[3])) + f * ((s1[0] + s1[1]) + (s1[2] + s1[3])));
        }
        BARRIER();
        wo_issue(3); wo_issue(4);
        int zw = ZR + (Q03 * 16 + l15) * ZROW + 256 * (g >> 1) + 32 * wq + 8 * (g & 1); OPAQUE(zw);
#pragma unroll
        for (int dt = 0; dt < 4; ++dt)
#pragma unroll
            for (int j = 0; j < NQ3; ++j) {
                u32x2 v = {pk2(acc3[dt][j][0] * rl[j], acc3[dt][j][1] * rl[j]), pk2(acc3[dt][j][2] * rl[j], acc3[dt][j][3] * rl[j])};
                *(AS3 u32x2*)(smem + zw + 128 * (dt & 1) + 16 * (dt >> 1) + j * ZT) = v;
            }

        SBAR();
        f32x4 acc4[7][NQ4];
#pragma unroll
        for (int nt = 0; nt < 7; ++nt)
#pragma unroll
            for (int j = 0; j < NQ4; ++j) acc4[nt][j] = f32x4{0, 0, 0, 0};
        int lane4 = lane_in; OPAQUE(lane4);
        const int l15b = lane4 & 15, gb = lane4 >> 4, hib = lane4 >> 5, qqb = l15b >> 2, pb = lane4 & 3;
        int zr = ZR + (Q04 * 16 + l15b) * ZROW + 256 * (gb & 1) + 128 * (gb >> 1); OPAQUE(zr);
        const int rr4 = 8 * (gb & 1) + qqb;
        const int fW4 = ((qqb >> 1) & 1) | ((gb & 1) << 1);
        int woff[7];
#pragma unroll
        for (int nt = 0; nt < 7; ++nt) woff[nt] = rr4 * 896 + (((14 * wq + 2 * nt + (pb >> 1)) ^ (fW4 << 1)) << 4) + 8 * (pb & 1);
#pragma unroll
        for (int m = 0; m < 8; ++m) {
            const int last = (m == 0) ? 20 : (2 * m + 2 + 16 > 31 ? 31 : 2 * m + 2 + 16);
            WAITV(outstanding_after(GRP, 16 + 2 * m + 1, last));
            BARRIER();
            if (m >= 1) { if (2 * m + 3 < 16) wo_issue(2 * m + 3); if (2 * m + 4 < 16) wo_issue(2 * m + 4); }
            lptr ws = smem + (hib ? wslot_off(2 * m + 1) : wslot_off(2 * m));
            bf16x8 a[7], b[NQ4];
#pragma unroll
            for (int j = 0; j < NQ4; ++j) b[j] = ld128(smem + zr + 16 * m + j * ZT);
#pragma unroll
            for (int nt = 0; nt < 7; ++nt) a[nt] = ldtr(ws + woff[nt], ws + woff[nt] + 4 * 896);
#pragma unroll
            for (int nt = 0; nt < 7; ++nt)
#pragma unroll
                for (int j = 0; j < NQ4; ++j) acc4[nt][j] = MFMA(a[nt], b[j], acc4[nt][j]);
        }
        const int pbase = (GRP == 0 ? wq * 18432 : 73728 + wq * 14336) + lane4 * 16;
        constexpr int NT4 = 7 * NQ4;
#pragma unroll
        for (int v = 0; v < (NT4 + 1) / 2; ++v) {
            const int u0 = 2 * v, u1 = (2 * v + 1 < NT4) ? 2 * v + 1 : 2 * v;
            const f32x4 t0 = acc4[u0 / NQ4][u0 % NQ4], t1 = acc4[u1 / NQ4][u1 % NQ4];
            u32x4 o = {pk2(t0[0], t0[1]), pk2(t0[2], t0[3]), pk2(t1[0], t1[1]), pk2(t1[2], t1[3])};
            __builtin_amdgcn_raw_buffer_store_b128(o, rsP, pbase, v * 1024, 16);
        }
    }
}

__global__ __launch_bounds__(512, 2) void mha_main(const short* __restrict__ x, const short* __restrict__ kc,
                                                   const short* __restrict__ vc, const short* __restrict__ wqp,
                                                   const short* __restrict__ wop, char* __restrict__ part) {
    __shared__ __attribute__((aligned(16))) char smem_[LDS_TOTAL];
    lptr smem = (lptr)smem_;
    const int bid = blockIdx.x;
    const int xcd = bid & 7, r = bid >> 3;
    const int b = (xcd & 1) * 8 + (r & 7), h = (xcd >> 1) * 4 + (r >> 3);
    const int bh = b * NH + h;
    const int tid = threadIdx.x, lane = tid & 63;
    const int w = __builtin_amdgcn_readfirstlane(tid >> 6);
    __amdgpu_buffer_rsrc_t rsX = __builtin_amdgcn_make_buffer_rsrc((void*)(x + (size_t)b * SQ * DM), 0, SQ * DM * 2, 0x00020000);
    __amdgpu_buffer_rsrc_t rsWq = __builtin_amdgcn_make_buffer_rsrc((void*)(wqp + h * DH), 0, (DM * NH * DH - h * DH) * 2, 0x00020000);
    __amdgpu_buffer_rsrc_t rsK = __builtin_amdgcn_make_buffer_rsrc((void*)(kc + (size_t)bh * SK * DH), 0, SK * DH * 2, 0x00020000);
    __amdgpu_buffer_rsrc_t rsV = __builtin_amdgcn_make_buffer_rsrc((void*)(vc + (size_t)bh * SK * DH), 0, SK * DH * 2, 0x00020000);
    __amdgpu_buffer_rsrc_t rsWo = __builtin_amdgcn_make_buffer_rsrc((void*)(wop + (size_t)h * DH * DM), 0, DH * DM * 2, 0x00020000);
    __amdgpu_buffer_rsrc_t rsP = __builtin_amdgcn_make_buffer_rsrc((void*)(part + (size_t)bh * PART_WG), 0, PART_WG, 0x00020000);
    if (w < 4) wave_main<0>(smem, w, lane, rsX, rsWq, rsK, rsV, rsWo, rsP);
    else wave_main<1>(smem, w - 4, lane, rsX, rsWq, rsK, rsV, rsWo, rsP);
}

__global__ __launch_bounds__(256) void mha_reduce(const u32x4* __restrict__ part, unsigned short* __restrict__ out) {
    const int gid = blockIdx.x * 256 + threadIdx.x;
    const int b = gid >> 13, gi = gid & 8191;
    const u32x4* p = part + (size_t)b * NH * (PART_WG / 16) + gi;
    u32x4 v[16];
#pragma unroll
    for (int h = 0; h < 16; ++h) v[h] = __builtin_nontemporal_load(p + h * (PART_WG / 16));
    float s[8];
#pragma unroll
    for (int e = 0; e < 8; ++e) s[e] = 0.f;
#pragma unroll
    for (int h = 0; h < 16; ++h)
#pragma unroll
        for (int e = 0; e < 4; ++e) {
            s[2 * e] += __builtin_bit_cast(float, v[h][e] << 16);
            s[2 * e + 1] += __builtin_bit_cast(float, v[h][e] & 0xffff0000u);
        }
    const int lane = gi & 63, blk = gi >> 6;
    int grp, wq, v_;
    if (blk < 72) { grp = 0; wq = blk / 18; v_ = blk % 18; } else { grp = 1; wq = (blk - 72) / 14; v_ = (blk - 72) % 14; }
    const int NQ = grp == 0 ? 5 : 4, Q0 = grp == 0 ? 0 : 5;
#pragma unroll
    for (int t = 0; t < 2; ++t) {
        const int u = 2 * v_ + t;
        if (u < 7 * NQ) {
            const int nt = u / NQ, j = u % NQ;
            const int q = (Q0 + j) * 16 + (lane & 15), n = 112 * wq + 16 * nt + 4 * (lane >> 4);
            u32x2 o = {pk2(s[4 * t], s[4 * t + 1]), pk2(s[4 * t + 2], s[4 * t + 3])};
            *(u32x2*)(out + ((size_t)(b * SQ + q) * DM + n)) = o;
        }
    }
}

extern "C" void kernel_launch(void* const* d_in, const int* in_sizes, int n_in,
                              void* d_out, int out_size, void* d_ws, size_t ws_size,
                              hipStream_t stream) {
    mha_main<<<256, 512, 0, stream>>>((const short*)d_in[0], (const short*)d_in[1], (const short*)d_in[2],
                                      (const short*)d_in[3], (const short*)d_in[4], (char*)d_ws);
    mha_reduce<<<512, 256, 0, stream>>>((const u32x4*)d_ws, (unsigned short*)d_out);
}
```

```cpp
#include <hip/hip_runtime.h>
#include <hip/hip_bf16.h>

typedef __attribute__((ext_vector_type(8))) short bf16x8;
typedef __attribute__((ext_vector_type(4))) short s16x4;
typedef __attribute__((ext_vector_type(4))) float f32x4;
typedef __attribute__((ext_vector_type(4))) unsigned u32x4;
typedef __attribute__((ext_vector_type(2))) unsigned u32x2;
typedef __attribute__((ext_vector_type(2))) __bf16 bf16x2;
#define AS3 __attribute__((address_space(3)))
#define DI __device__ __forceinline__
typedef AS3 char* lptr;
typedef __attribute__((address_space(1))) unsigned long long gu64;

constexpr int SQ = 144, DM = 448, NH = 16, SK = 512, DH = 256;
constexpr int ST_BYTES = 51200;
constexpr int ST_X = 0, ST_W = 18432;
constexpr int SLOT = 16384;
constexpr int ZR = 81920;
constexpr int ZROW = 528;
constexpr int ZT = 16 * ZROW;
constexpr int ZBYTES = SQ * ZROW;
constexpr int PBUF = 18432;
constexpr int PB0 = 0, PB1 = ZBYTES - PBUF;
constexpr int SLOT56 = ZR + PBUF;
constexpr int SMALL = ZR + ZBYTES;
constexpr int MAXA1 = SMALL;
constexpr int MAXA2 = SMALL + 576;
constexpr int SUMA = SMALL + 1152;
constexpr int CNTA = SUMA + 4608;
constexpr int TAGA = CNTA + 8;
constexpr int RDYA = CNTA + 4;
constexpr int LDS_TOTAL = 163840;
static_assert(TAGA + 8 <= LDS_TOTAL, "lds");
static_assert(PBUF + 2 * SLOT <= PB1, "lds z");
static_assert(6 * ZT <= PB1, "early O tiles must not touch the odd P buffer");
constexpr int PART_WG = 131072;

DI constexpr int vslot_off(int s) { return (s % 7) < 5 ? (s % 7) * SLOT : SLOT56 + ((s % 7) - 5) * SLOT; }
DI constexpr int wslot_off(int t) { return ((t + 2) % 5) * SLOT; }
DI constexpr int pbuf_off(int i) { return (i & 1) ? PB1 : PB0; }

#define WAITV(n) asm volatile("s_waitcnt vmcnt(%0)" ::"n"(n) : "memory")
#define BARRIER() asm volatile("s_waitcnt lgkmcnt(0)\n\ts_barrier" ::: "memory")
#define SBAR() __builtin_amdgcn_sched_barrier(0)
#define OPAQUE(x) asm volatile("" : "+v"(x))
#define ACCWAIT() do { asm volatile("s_nop 7\n\ts_nop 1"); SBAR(); } while (0)

template <int I> struct IC { static constexpr int value = I; constexpr operator int() const { return I; } };
template <int B, int E, class F> DI void sfor(F&& f) { if constexpr (B < E) { f(IC<B>{}); sfor<B + 1, E>(f); } }

DI void mfma(bool first, f32x4& c, const bf16x8& a, const bf16x8& b) {
    if (first) asm volatile("v_mfma_f32_16x16x32_bf16 %0, %1, %2, 0" : "=v"(c) : "v"(a), "v"(b));
    else asm volatile("v_mfma_f32_16x16x32_bf16 %0, %1, %2, %0" : "+v"(c) : "v"(a), "v"(b));
}
DI int fresh_lane() {
    int l;
    asm volatile("v_mbcnt_lo_u32_b32 %0, -1, 0\n\tv_mbcnt_hi_u32_b32 %0, -1, %0" : "=v"(l));
    return l;
}
DI bf16x8 ld128(lptr p) { return *(AS3 const bf16x8*)p; }
DI s16x4 ldtr1(lptr p) { return __builtin_amdgcn_ds_read_tr16_b64_v4i16((AS3 s16x4*)p); }
DI bf16x8 cat(const s16x4& a, const s16x4& b) { return __builtin_shufflevector(a, b, 0, 1, 2, 3, 4, 5, 6, 7); }
DI unsigned pk2(float a, float b) {
    bf16x2 v = {(__bf16)a, (__bf16)b};
    return __builtin_bit_cast(unsigned, v);
}
DI float xsum(float s) {
    unsigned u = __builtin_bit_cast(unsigned, s);
    auto r = __builtin_amdgcn_permlane32_swap(u, u, false, false);
    s = __builtin_bit_cast(float, (unsigned)r[0]) + __builtin_bit_cast(float, (unsigned)r[1]);
    u = __builtin_bit_cast(unsigned, s);
    auto r2 = __builtin_amdgcn_permlane16_swap(u, u, false, false);
    return __builtin_bit_cast(float, (unsigned)r2[0]) + __builtin_bit_cast(float, (unsigned)r2[1]);
}
DI void dma16(__amdgpu_buffer_rsrc_t rs, lptr dst, int voff, int soff, const int aux) {
    const unsigned d = (unsigned)(size_t)dst;
    if (aux == 2)
        asm volatile("s_mov_b32 m0, %0\n\ts_nop 0\n\tbuffer_load_dwordx4 %1, %2, %3 offen nt lds" ::"s"(d), "v"(voff), "s"(rs), "s"(soff) : "m0", "memory");
    else
        asm volatile("s_mov_b32 m0, %0\n\ts_nop 0\n\tbuffer_load_dwordx4 %1, %2, %3 offen lds" ::"s"(d), "v"(voff), "s"(rs), "s"(soff) : "m0", "memory");
}

#ifndef NVY_
#define NVY_ 5
#endif
constexpr int NVY = NVY_;
constexpr int cnt_of(int grp, int id) { return grp == 0 ? 4 : 0; }
constexpr int outstanding_after(int grp, int need, int last) {
    int n = 0;
    for (int i = need + 1; i <= last; ++i) n += cnt_of(grp, i);
    return n;
}
constexpr int x_wait4(int m1) {
    int n = 0; bool seen = (2 * m1 + 1 <= 4);
    if (seen) { for (int t = 2 * m1 + 2; t <= 4; ++t) n += 4; }
    for (int t = 0; t < 2 * m1; ++t) {
        const int NT = t < 8 ? 4 : 3, m = t >> 1;
        for (int half = 0; half < 2; ++half) {
            if (t == 2 * m1 - 1 && half == 1) break;
            const int cnt = 2 * NT;
            for (int i = 0; i < cnt; ++i) {
                int pc = -1;
                if ((t & 1) == 0 && m >= 1) {
                    if (half == 0 && i >= 2) pc = i - 2;
                    if (half == 1 && 2 * NT - 2 + i < 8) pc = 2 * NT - 2 + i;
                }
                if (pc >= 0) {
                    const int w = 2 * m + 3 + (pc >> 2);
                    if (w <= 15) { if (seen) ++n; if (w == 2 * m1 + 1 && (pc & 3) == 3) { seen = true; n = 0; } }
                }
                if (t >= 8 && i == 1 && 2 * (t - 8) + half < 8) { if (seen) ++n; }
            }
        }
    }
    return n;
}

#define PW 7
#define PSLEEP 8
#define KYA 12
#define KYB 6
#define KYC 10
template <int GRP>
DI void wave_main(lptr smem, const int wq, const int lane_in, const int rot1, const int rot4, gu64* flag1,
                  __amdgpu_buffer_rsrc_t rsX, __amdgpu_buffer_rsrc_t rsWq, __amdgpu_buffer_rsrc_t rsK,
                  __amdgpu_buffer_rsrc_t rsV, __amdgpu_buffer_rsrc_t rsWo, __amdgpu_buffer_rsrc_t rsP) {
    constexpr int NQ3 = GRP == 0 ? 3 : 6, Q03 = GRP == 0 ? 0 : 3;
    constexpr int NQ4 = GRP == 0 ? 4 : 5, Q04 = GRP == 0 ? 0 : 4;
    constexpr float CS = 0.0625f * 1.4426950408889634f;
    const int w = GRP * 4 + wq;
    bf16x8 kf[4][8];
#define KLOAD(jt, s) kf[jt][s] = __builtin_bit_cast(bf16x8, __builtin_amdgcn_raw_buffer_load_b128(rsK, kvoff + 128 * ((s) >> 1), (jt) * 32768 + ((s) & 1) * 4096, 2))
    auto kfix = [&](int jt, int cb, int d) {
        u32x4 A = __builtin_bit_cast(u32x4, kf[jt][2 * cb]), B = __builtin_bit_cast(u32x4, kf[jt][2 * cb + 1]);
        const unsigned a = A[d], b = B[d];
        A[d] = __builtin_amdgcn_update_dpp(a, b, 0x128, 0xf, 0xc, false);
        B[d] = __builtin_amdgcn_update_dpp(b, a, 0x128, 0xf, 0x3, false);
        kf[jt][2 * cb] = __builtin_bit_cast(bf16x8, A); kf[jt][2 * cb + 1] = __builtin_bit_cast(bf16x8, B);
    };

    {
        int lane = fresh_lane();
        const int l15 = lane & 15, g = lane >> 4, hi = lane >> 5, qq = l15 >> 2, p = lane & 3;
        if (GRP == 1) {
            const int kvoff = (16 * wq + (lane & 7)) * 512 + 16 * (lane >> 3);
            const int rW = 2 * wq + hi;
            const int wv0 = rW * 8192 + (((lane & 31) ^ ((rW & 3) << 2)) << 4);
            const int wv1 = wv0 ^ 32;
            const int xv = (lane >> 3) * 896 + (((lane & 7) ^ (((lane >> 4) + 4 * (wq & 1)) & 7)) << 4);
            if (wq == 0) {
                if (lane < 36) {
                    *(AS3 f32x4*)(smem + MAXA1 + lane * 16) = f32x4{-INFINITY, -INFINITY, -INFINITY, -INFINITY};
                    *(AS3 f32x4*)(smem + MAXA2 + lane * 16) = f32x4{-INFINITY, -INFINITY, -INFINITY, -INFINITY};
                }
                if (lane == 36) *(AS3 unsigned*)(smem + CNTA) = 0u;
                if (lane == 37) *(AS3 unsigned*)(smem + RDYA) = 0u;
            }
            auto group_issue = [&](auto i_) {
                constexpr int i = decltype(i_)::value;
                lptr st = smem + (i % 3) * ST_BYTES;
                int c = i + rot1; c = c >= 7 ? c - 7 : c;
#pragma unroll
                for (int j = 0; j < 5; ++j) {
                    int n = wq + 4 * j;
                    if (j == 4) n = 16 + (wq & 1);
                    dma16(rsX, st + ST_X + n * 1024, xv, n * 8 * 896 + 128 * c, 0);
                }
#pragma unroll
                for (int j = 0; j < 8; ++j)
                    dma16(rsWq, st + ST_W + (wq + 4 * j) * 1024, (j & 1) ? wv1 : wv0, (64 * c + 8 * j) * 8192, 0);
            };
#define KY(id) KLOAD((((id) >> 4) << 1) | (((id) >> 1) & 1), (((id) >> 2) & 3) * 2 + ((id) & 1))
            group_issue(IC<0>{});
            sfor<0, 7>([&](auto i_) { constexpr int i = decltype(i_)::value;
                if constexpr (i + 1 <= 6) {
                    group_issue(IC<i + 1>{});
                    if constexpr (i + 1 == 6) {
#pragma unroll
                        for (int u = 0; u < KYA; ++u) KY(u);
                        WAITV(13 + KYA);
                    } else WAITV(13);
                } else {
#pragma unroll
                    for (int u = KYA; u < KYA + KYB; ++u) KY(u);
                    WAITV(KYA + KYB);
                }
                BARRIER();
            });
#pragma unroll
            for (int u = KYA + KYB; u < KYA + KYB + KYC; ++u) KY(u);
        } else {
            const int fV = (qq << 2) | ((g & 1) << 1);
            int aoff[4], az[4];
#pragma unroll
            for (int dt = 0; dt < 4; ++dt) aoff[dt] = ST_W + (8 * g + qq) * 512 + (((8 * wq + 2 * dt + (p >> 1)) ^ fV) << 4) + 8 * (p & 1);
            const int fX = l15 >> 1;
            int boff[2], bz[2];
#pragma unroll
            for (int ks = 0; ks < 2; ++ks) boff[ks] = ST_X + l15 * 128 + (((4 * ks + g) ^ fX) << 4);
            auto set_stage = [&](int i) {
                int sb = (i % 3) * ST_BYTES; asm volatile("" : "+s"(sb));
#pragma unroll
                for (int dt = 0; dt < 4; ++dt) az[dt] = aoff[dt] + sb;
                bz[0] = boff[0] + sb; bz[1] = boff[1] + sb;
            };
            f32x4 acc1[4][9];
            s16x4 ah[2][4][2];
            bf16x8 b[9];
            auto ld_head = [&](int t, int u) {
                if (u == 0) b[0] = ld128(smem + bz[t & 1]);
                else if (u <= 8) ah[t & 1][(u - 1) >> 1][(u - 1) & 1] = ldtr1(smem + az[(u - 1) >> 1] + (t & 1) * 16384 + ((u - 1) & 1) * 2048);
                else b[u - 8] = ld128(smem + bz[t & 1] + (u - 8) * 2048);
            };
            BARRIER();
            set_stage(0);
#pragma unroll
            for (int u = 0; u < 13; ++u) ld_head(0, u);
            SBAR();
            sfor<0, 12>([&](auto t_) { constexpr int t = decltype(t_)::value;
#pragma unroll
                for (int i = 0; i < 20; ++i) {
                    mfma(t == 0, acc1[i & 3][i >> 2], cat(ah[t & 1][i & 3][0], ah[t & 1][i & 3][1]), b[i >> 2]);
                    if (i < 8 && (i & 1) == 0) b[5 + (i >> 1)] = ld128(smem + bz[t & 1] + (5 + (i >> 1)) * 2048);
                    SBAR();
                }
                if (t & 1) { BARRIER(); set_stage((t + 1) >> 1); SBAR(); }
#pragma unroll
                for (int i = 0; i < 16; ++i) {
                    mfma(t == 0, acc1[i & 3][5 + (i >> 2)], cat(ah[t & 1][i & 3][0], ah[t & 1][i & 3][1]), b[5 + (i >> 2)]);
                    if (i < 13) ld_head(t + 1, i);
                    SBAR();
                }
            });
            int lz = fresh_lane();
            int zw = ZR + (lz & 15) * ZROW + 16 * (lz >> 5) + 32 * wq + 8 * ((lz >> 4) & 1); OPAQUE(zw);
            bf16x8 c1[3];
            auto qwrite = [&](int j, int dt) {
                u32x2 v = {pk2(acc1[dt][j][0], acc1[dt][j][1]), pk2(acc1[dt][j][2], acc1[dt][j][3])};
                *(AS3 u32x2*)(smem + zw + 256 * (dt & 1) + 128 * (dt >> 1) + j * ZT) = v;
            };

            sfor<0, 9>([&](auto j_) { constexpr int j = decltype(j_)::value;
#pragma unroll
                for (int i = 0; i < 8; ++i) {
                    const int ks = i >> 2, dt = i & 3;
                    if (j == 0 && ks == 1) {
                    }
                    mfma(false, acc1[dt][j], cat(ah[ks][dt][0], ah[ks][dt][1]), ks == 0 ? b[j] : c1[j % 3]);
                    if (j == 0 && i < 4) {
                        ah[1][i][0] = ldtr1(smem + az[i] + 16384);
                        ah[1][i][1] = ldtr1(smem + az[i] + 16384 + 2048);
                        if (i < 3) c1[i] = ld128(smem + bz[1] + i * 2048);
                    }
                    if (j < 4 && i == 5) b[5 + j] = ld128(smem + bz[0] + (5 + j) * 2048);
                    if (j >= 1 && j + 2 < 9 && i == 0) c1[(j + 2) % 3] = ld128(smem + bz[1] + (j + 2) * 2048);
                    if (j >= 2 && i >= 2 && i < 6) qwrite(j - 2, i - 2);
                    SBAR();
                }
            });
            ACCWAIT();
#pragma unroll
            for (int dt = 0; dt < 4; ++dt) { qwrite(7, dt); qwrite(8, dt); }
        }
        BARRIER();
    }

    f32x4 S[4][9];
    unsigned Pk[4][9][2];
    float lsum[9];
    {
        int lane = fresh_lane();
        const int l15 = lane & 15, g = lane >> 4, hi = lane >> 5;
        int zr = ZR + l15 * ZROW + 256 * (g & 1) + 128 * (g >> 1); OPAQUE(zr);
        int sm = SMALL + l15 * 4; OPAQUE(sm);
        int sms = SUMA + l15 * 32 + w * 4; OPAQUE(sms);
        {
            bf16x8 bq[3][3];
            int zr2 = zr + 6 * ZT; OPAQUE(zr2);
            auto ldq = [&](int st, int u) {
                const int s1 = (st % 24) / 3, jb1 = st % 3;
                bq[st % 3][u] = (jb1 == 2) ? ld128(smem + zr2 + 16 * s1 + u * ZT) : ld128(smem + zr + 16 * s1 + (3 * jb1 + u) * ZT);
            };
#define YP2 2
#define XP2 0
            if (GRP == 1) __builtin_amdgcn_s_setprio(YP2); else __builtin_amdgcn_s_setprio(XP2);
            const int kvoff = ((GRP == 0 ? 256 : 0) + 16 * wq + (lane & 7)) * 512 + 16 * (lane >> 3);
            if (GRP == 0) {
                __builtin_amdgcn_s_sleep(4);
#pragma unroll
                for (int u = 0; u < 32; ++u) KY(u);
            } else {
#pragma unroll
                for (int u = KYA + KYB + KYC; u < 32; ++u) KY(u);
            }
            const int rVx = 2 * wq + hi;
            const int vx0 = rVx * 512 + (((lane & 31) ^ ((rVx & 3) << 2)) << 4);
#pragma unroll
            for (int u = 0; u < 6; ++u) ldq(u / 3, u % 3);
#pragma unroll
            for (int d = 0; d < 4; ++d) { kfix(0, 0, d); kfix(1, 0, d); }
            SBAR();
            asm volatile("s_nop 1");
            SBAR();
            sfor<0, 48>([&](auto st_) { constexpr int st = decltype(st_)::value;
                const int hf = st / 24, s = (st % 24) / 3, jb = st % 3;
#pragma unroll
                for (int i = 0; i < 6; ++i) {
                    const int u = i >> 1, jt = i & 1;
                    mfma(s == 0, S[2 * hf + jt][3 * jb + u], kf[2 * hf + jt][s], bq[st % 3][u]);
                    if (st + 2 < 48 && (i & 1) == 0) ldq(st + 2, i >> 1);
                    if (GRP == 0 && st >= 26 && st < 46 && i == 5) {
                        constexpr int u = st - 26;
                        dma16(rsV, smem + vslot_off(u >> 2) + (wq + 4 * (u & 3)) * 1024, vx0 ^ ((u & 1) << 5), (u >> 2) * 16384 + (u & 3) * 4096, 2);
                    }
                    {
                        constexpr int blk = st / 6, ph = st % 6;
                        if (blk + 1 < 8 && ph < 4 && (i == 1 || i == 3)) {
                            constexpr int nb = blk + 1;
                            kfix(2 * (nb / 4) + (i == 3 ? 1 : 0), nb % 4, ph);
                        }
                    }
                    SBAR();
                }
                if (GRP == 0 && st == 25) {
#pragma unroll
                    for (int s2 = 0; s2 < 8; ++s2) { OPAQUE(kf[2][s2]); OPAQUE(kf[3][s2]); }
                    SBAR();
                }

            });
            ACCWAIT();
        }
#pragma unroll
        for (int j = 0; j < 9; ++j) {
            float m = S[0][j][0];
#pragma unroll
            for (int jt = 0; jt < 4; ++jt)
#pragma unroll
                for (int r = 0; r < 4; ++r) m = fmaxf(m, S[jt][j][r]);
            asm volatile("ds_max_f32 %0, %1 offset:%2" ::"v"((unsigned)(size_t)(smem + sm)), "v"(m), "n"((GRP == 1 ? 0 : 576) + 64 * j) : "memory");
        }
        if (GRP == 1) {
            {
                asm volatile("s_waitcnt lgkmcnt(0)" ::: "memory");
                const unsigned ca = (unsigned)(size_t)(smem + CNTA);
                if (lane == 0) asm volatile("ds_add_u32 %0, %1" ::"v"(ca), "v"(1u) : "memory");
                unsigned cv;
                do {
                    asm volatile("ds_read_b32 %0, %1\n\ts_waitcnt lgkmcnt(0)" : "=v"(cv) : "v"(ca) : "memory");
                    cv = __builtin_amdgcn_readfirstlane(cv);
                } while (cv < 4u);
                __builtin_amdgcn_s_setprio(0);
            }
            const int rVy = 2 * wq + hi;
            const int vy0 = rVy * 512 + (((lane & 31) ^ ((rVy & 3) << 2)) << 4);
            const int vy1 = vy0 ^ 32;
            auto v_issue1 = [&](int u) {
                const int s = u >> 2, j = u & 3;
                dma16(rsV, smem + vslot_off(s) + (wq + 4 * j) * 1024, (j & 1) ? vy1 : vy0, s * 16384 + j * 4096, 2);
            };
#pragma unroll
            for (int j = 0; j < 9; ++j) {
                const float mk = *(AS3 const float*)(smem + sm + 64 * j) * CS;
                float ls = 0.f;
#pragma unroll
                for (int jt = 0; jt < 4; ++jt) {
                    float e0 = __builtin_amdgcn_exp2f(fmaf(S[jt][j][0], CS, -mk));
                    float e1 = __builtin_amdgcn_exp2f(fmaf(S[jt][j][1], CS, -mk));
                    float e2 = __builtin_amdgcn_exp2f(fmaf(S[jt][j][2], CS, -mk));
                    float e3 = __builtin_amdgcn_exp2f(fmaf(S[jt][j][3], CS, -mk));
                    ls += (e0 + e1) + (e2 + e3);
                    Pk[jt][j][0] = pk2(e0, e1); Pk[jt][j][1] = pk2(e2, e3);
                    OPAQUE(Pk[jt][j][0]); OPAQUE(Pk[jt][j][1]);
                }
                ls = xsum(ls);
                *(AS3 float*)(smem + sms + 512 * j) = ls;
            }
        }
        BARRIER();
    }

    f32x4 acc3[4][NQ3];
    {
        int lane = fresh_lane();
        const int l15 = lane & 15, g = lane >> 4, hi = lane >> 5, qq = l15 >> 2, p = lane & 3;
        const int fV = (qq << 2) | ((g & 1) << 1);
        int aoff[4];
#pragma unroll
        for (int dt = 0; dt < 4; ++dt) aoff[dt] = (8 * g + qq) * 512 + (((8 * wq + 2 * dt + (p >> 1)) ^ fV) << 4) + 8 * (p & 1);
        const int fX = l15 >> 1;
        int boff[2][2];
#pragma unroll
        for (int pb = 0; pb < 2; ++pb)
#pragma unroll
            for (int ks = 0; ks < 2; ++ks) {
                if (GRP == 0 && pb == 1) boff[1][ks] = boff[0][ks] + PB1;
                else { boff[pb][ks] = ZR + pbuf_off(pb) + (Q03 * 16 + l15) * 128 + (((4 * ks + g) ^ fX) << 4); OPAQUE(boff[pb][ks]); }
            }
        const int rVy = 2 * wq + hi;
        const int vy0 = rVy * 512 + (((lane & 31) ^ ((rVy & 3) << 2)) << 4);
        const int vy1 = vy0 ^ 32;
        auto v_issue = [&](int s) {
#pragma unroll
            for (int j = 0; j < 4; ++j)
                dma16(rsV, smem + vslot_off(s) + (wq + 4 * j) * 1024, (j & 1) ? vy1 : vy0, s * 16384 + j * 4096, 2);
        };
        int wov[4];
        auto wov_init = [&]() {
            int lw = fresh_lane();
            const int r0 = 2 * wq + (lw >> 5);
#pragma unroll
            for (int jp = 0; jp < 2; ++jp) {
                const int ch = (lw & 31) ^ (((r0 & 3) << 2) | (jp << 1));
                wov[jp] = r0 * 896 + ch * 16;
                wov[2 + jp] = r0 * 896 + (ch > 23 ? 23 : ch) * 16;
            }
        };
        auto wo_issue1 = [&](int t, int j) {
            dma16(rsWo, smem + wslot_off(t) + (wq + 4 * j) * 1024, wov[(t >> 3) * 2 + (j & 1)],
                  (32 * (((t & 7) + rot4) & 7) + 8 * j) * 896 + (t >> 3) * 512, 0);
        };
        auto wo_issue = [&](int t) {
#pragma unroll
            for (int j = 0; j < 4; ++j) wo_issue1(t, j);
        };
        auto seq_issue = [&](int id) { if (GRP == 0) { if (id < 16) v_issue(id); else if (id < 32) wo_issue(id - 16); } };
        auto seq_issue1 = [&](int id, int j) {
            if (GRP == 0) {
                if (id < 16) dma16(rsV, smem + vslot_off(id) + (wq + 4 * j) * 1024, (j & 1) ? vy1 : vy0, id * 16384 + j * 4096, 2);
                else if (id < 32) wo_issue1(id - 16, j);
            }
        };
        int pwoff[2];
        auto pw_init = [&]() {
            int lw = fresh_lane();
#pragma unroll
            for (int pb = 0; pb < 2; ++pb) { pwoff[pb] = ZR + pbuf_off(pb) + (lw & 15) * 128 + (((2 * wq + (lw >> 5)) ^ ((lw & 15) >> 1)) << 4) + 8 * ((lw >> 4) & 1); OPAQUE(pwoff[pb]); }
        };
        if (GRP == 1) pw_init();
        auto pwrite1 = [&](int i, int j) {
            u32x2 v = {Pk[i & 3][j][0], Pk[i & 3][j][1]};
            *(AS3 u32x2*)(smem + pwoff[i & 1] + j * 2048) = v;
        };
        auto pwrite = [&](int i) {
#pragma unroll
            for (int j = 0; j < 9; ++j) pwrite1(i, j);
        };
        int sm = SMALL + l15 * 4; OPAQUE(sm);
        int sms = SUMA + l15 * 32; OPAQUE(sms);
        float mk[9];
        auto xexp = [&](int u0, int u1) {
#pragma unroll
            for (int u = u0; u < u1; ++u) {
                const int jt = u / 9, j = u % 9;
                if (jt == 0) {
                    const float m1 = *(AS3 const float*)(smem + sm + 64 * j), m2 = *(AS3 const float*)(smem + sm + 576 + 64 * j);
                    mk[j] = fmaxf(m1, m2) * CS;
                }
                float e0 = __builtin_amdgcn_exp2f(fmaf(S[jt][j][0], CS, -mk[j]));
                float e1 = __builtin_amdgcn_exp2f(fmaf(S[jt][j][1], CS, -mk[j]));
                float e2 = __builtin_amdgcn_exp2f(fmaf(S[jt][j][2], CS, -mk[j]));
                float e3 = __builtin_amdgcn_exp2f(fmaf(S[jt][j][3], CS, -mk[j]));
                const float ls = (e0 + e1) + (e2 + e3);
                lsum[j] = jt == 0 ? ls : lsum[j] + ls;
                Pk[jt][j][0] = pk2(e0, e1); Pk[jt][j][1] = pk2(e2, e3);
                OPAQUE(Pk[jt][j][0]); OPAQUE(Pk[jt][j][1]); OPAQUE(lsum[j]);
                if (jt == 3) {
                    *(AS3 float*)(smem + sms + w * 4 + 512 * j) = xsum(lsum[j]);
                }
            }
        };
        float xe[4];
        auto xpart = [&](int u, int part) {
            const int jt = u / 9, j = u % 9;
            if (part == 0) {
#pragma unroll
                for (int r = 0; r < 4; ++r) { xe[r] = fmaf(S[jt][j][r], CS, -mk[j]); OPAQUE(xe[r]); }
            } else if (part == 1) {
#pragma unroll
                for (int r = 0; r < 4; ++r) { xe[r] = __builtin_amdgcn_exp2f(xe[r]); OPAQUE(xe[r]); }
            } else if (part == 2) {
                lsum[j] += (xe[0] + xe[1]) + (xe[2] + xe[3]); OPAQUE(lsum[j]);
            } else {
                Pk[jt][j][0] = pk2(xe[0], xe[1]); Pk[jt][j][1] = pk2(xe[2], xe[3]);
                OPAQUE(Pk[jt][j][0]); OPAQUE(Pk[jt][j][1]);
                if (jt == 3) *(AS3 float*)(smem + sms + w * 4 + 512 * j) = xsum(lsum[j]);
            }
            SBAR();
        };
#define XPRIO 2
        if (GRP == 0) __builtin_amdgcn_s_setprio(XPRIO);
        if (GRP == 1) pwrite(0);
#define LAST_BEFORE(i) ((i) == 0 ? NVY - 1 : (i) <= 1 ? 6 : ((i) <= 7 ? 2 * (i) + 4 : 18))
        auto rescale = [&]() {
            ACCWAIT();
#pragma unroll
            for (int j = 0; j < NQ3; ++j) {
                const float m1 = *(AS3 const float*)(smem + sm + 64 * (Q03 + j)), m2 = *(AS3 const float*)(smem + sm + 576 + 64 * (Q03 + j));
                const float f = __builtin_amdgcn_exp2f((m1 - fmaxf(m1, m2)) * CS);
#pragma unroll
                for (int dt = 0; dt < 4; ++dt) { acc3[dt][j] *= f; OPAQUE(acc3[dt][j]); }
            }
            SBAR();
            asm volatile("s_nop 1");
            SBAR();
        };
        int zw;
        auto zw_init = [&]() {
            int lw = fresh_lane();
            zw = ZR + (Q03 * 16 + (lw & 15)) * ZROW + 256 * (lw >> 5) + 32 * wq + 8 * ((lw >> 4) & 1); OPAQUE(zw);
        };
        float rl[NQ3];
        auto normalisers = [&]() {
            zw_init();
#pragma unroll
            for (int j = 0; j < NQ3; ++j) {
                const float m1 = *(AS3 const float*)(smem + sm + 64 * (Q03 + j)), m2 = *(AS3 const float*)(smem + sm + 576 + 64 * (Q03 + j));
                const float f = __builtin_amdgcn_exp2f((m1 - fmaxf(m1, m2)) * CS);
                f32x4 s0 = *(AS3 const f32x4*)(smem + sms + (Q03 + j) * 512);
                f32x4 s1 = *(AS3 const f32x4*)(smem + sms + (Q03 + j) * 512 + 16);
                rl[j] = 1.0f / (((s0[0] + s0[1]) + (s0[2] + s0[3])) + f * ((s1[0] + s1[1]) + (s1[2] + s1[3])));
                OPAQUE(rl[j]);
            }
        };
        auto owrite = [&](int j, int dt) {
            u32x2 v = {pk2(acc3[dt][j][0] * rl[j], acc3[dt][j][1] * rl[j]), pk2(acc3[dt][j][2] * rl[j], acc3[dt][j][3] * rl[j])};
            *(AS3 u32x2*)(smem + zw + 128 * (dt & 1) + 16 * (dt >> 1) + j * ZT) = v;
        };
        if (GRP == 0) {

            sfor<0, 8>([&](auto i_) { constexpr int i = decltype(i_)::value;
                WAITV(outstanding_after(GRP, 2 * i + 1, LAST_BEFORE(i)));
                BARRIER();
                s16x4 ah[2][4][2];
                bf16x8 b[2][NQ3];
                auto ldk = [&](int ks) {
                    lptr vs = smem + vslot_off(2 * i + ks);
#pragma unroll
                    for (int dt = 0; dt < 4; ++dt) { ah[ks][dt][0] = ldtr1(vs + aoff[dt]); ah[ks][dt][1] = ldtr1(vs + aoff[dt] + 2048); }
#pragma unroll
                    for (int j = 0; j < NQ3; ++j) b[ks][j] = ld128(smem + boff[i & 1][ks] + j * 2048);
                    SBAR();
                };
                auto mf4 = [&](int ks, int j) {
#pragma unroll
                    for (int dt = 0; dt < 4; ++dt) { mfma(i == 0 && ks == 0, acc3[dt][j], cat(ah[ks][dt][0], ah[ks][dt][1]), b[ks][j]); SBAR(); }
                };
                constexpr int id0 = (i == 0) ? 5 : 2 * i + 5;
                auto refill = [&](int u) { if (i <= 6) { seq_issue1(id0 + (u >> 2), u & 3); SBAR(); } };
                if (i > 0) ldk(0);
                if (i == 0) { if (NVY < 4) seq_issue(3); if (NVY < 5) seq_issue(4); }
                if (i == 4) rescale();
                if (i == 0) {
                    xexp(0, 6); SBAR();
                    ldk(0);
#pragma unroll
                    for (int j = 6; j < 9; ++j) {
                        const float m1 = *(AS3 const float*)(smem + sm + 64 * j), m2 = *(AS3 const float*)(smem + sm + 576 + 64 * j);
                        mk[j] = fmaxf(m1, m2) * CS; lsum[j] = 0.f;
                    }
                    SBAR();
                }
                if (i == 3) { pw_init(); SBAR(); }
                if (i == 7) { normalisers(); SBAR(); }
#pragma unroll
                for (int u = 0; u < 6; ++u) {
                    if (i == 4 && u == 5) { wov_init(); SBAR(); }
#pragma unroll
                    for (int dt = 0; dt < 4; ++dt) {
                        mfma(i == 0 && u < 3, acc3[dt][u % 3], cat(ah[u / 3][dt][0], ah[u / 3][dt][1]), b[u / 3][u % 3]); SBAR();
                        if (i > 0 && i < 6) xpart(6 * i + u, dt);
                        if (i >= 3 && i < 7 && u < 5 && dt < 2 && 2 * u + dt < 9) { pwrite1(i + 1, 2 * u + dt); SBAR(); }
                        if (u >= 2 && dt >= 2) refill(2 * (u - 2) + (dt - 2));
                    }
                    if (u == (i <= 2 ? 2 : 1)) ldk(1);
                }
            });
            ACCWAIT();
#pragma unroll
            for (int j = 0; j < NQ3; ++j)
#pragma unroll
                for (int dt = 0; dt < 4; ++dt) owrite(j, dt);
        } else {
            s16x4 ah[2][4][2];
            bf16x8 b[NQ3];
            auto ld_head = [&](int t, int u) {
                lptr vs = smem + vslot_off(t);
                if (u == 0) b[0] = ld128(smem + boff[(t >> 1) & 1][t & 1]);
                else if (u <= 8) ah[t & 1][(u - 1) >> 1][(u - 1) & 1] = ldtr1(vs + aoff[(u - 1) >> 1] + ((u - 1) & 1) * 2048);
                else b[u - 8] = ld128(smem + boff[(t >> 1) & 1][t & 1] + (u - 8) * 2048);
            };
            BARRIER();
#pragma unroll
            for (int u = 0; u < 11; ++u) ld_head(0, u);
            SBAR();

            sfor<0, 16>([&](auto t_) { constexpr int t = decltype(t_)::value;
                if (t == 8) rescale();
                if (t == 15) { normalisers(); SBAR(); }
#pragma unroll
                for (int i = 0; i < 12; ++i) {
                    mfma(t == 0, acc3[i & 3][i >> 2], cat(ah[t & 1][i & 3][0], ah[t & 1][i & 3][1]), b[i >> 2]);
                    if (i < 3) b[3 + i] = ld128(smem + boff[(t >> 1) & 1][t & 1] + (3 + i) * 2048);
                    else if ((t & 1) == 0 && (t >> 1) < 3) pwrite1((t >> 1) + 1, i - 3);
                    SBAR();
                }
                if (t + 1 < 16 && (t & 1)) {
                    const int i = (t + 1) >> 1;
                    BARRIER();
                }
#pragma unroll
                for (int i = 0; i < 12; ++i) {
                    mfma(t == 0, acc3[i & 3][3 + (i >> 2)], cat(ah[t & 1][i & 3][0], ah[t & 1][i & 3][1]), b[3 + (i >> 2)]);
                    if (t + 1 < 16) { if (i < 11) ld_head(t + 1, i); }
                    else owrite(i >> 2, i & 3);
                    SBAR();
                }
            });
        }
#define XPRIO4 0
        if (GRP == 0) __builtin_amdgcn_s_setprio(XPRIO4);
        BARRIER();
        seq_issue(19); seq_issue(20);
        if (GRP == 1) {
#pragma unroll
            for (int j = 3; j < 6; ++j)
#pragma unroll
                for (int dt = 0; dt < 4; ++dt) owrite(j, dt);
        }

        SBAR();
        int lane4 = fresh_lane();
        const int l15b = lane4 & 15, gb = lane4 >> 4, qqb = l15b >> 2, pb = lane4 & 3;
        int zr = ZR + (Q04 * 16 + l15b) * ZROW + 256 * (gb & 1) + 128 * (gb >> 1); OPAQUE(zr);
        const int fV4 = (qqb << 2) | ((gb & 1) << 1);
        int wo4[4];
        auto wo4_init = [&](int pass) {
#pragma unroll
            for (int nt = 0; nt < 4; ++nt)
                wo4[nt] = (8 * gb + qqb) * 512 + ((((pass ? 6 : 8) * wq + 2 * nt + (pb >> 1)) ^ fV4) << 4) + 8 * (pb & 1);
        };
        wo4_init(0);
        {
            f32x4 acc4[2][4][NQ4];
            s16x4 ah[2][4][2];
            bf16x8 b[NQ4];
            int zo[2];
            constexpr int NB1 = 2 * NQ4;
            constexpr int NB2 = (3 * NQ4 + 1) / 2;
            const int pbase1 = (GRP == 0 ? wq * 8 : 32 + wq * 10) * 1024 + lane4 * 16;
            const int pbase2 = (72 + (GRP == 0 ? wq * 6 : 24 + wq * 8)) * 1024 + lane4 * 16;
            auto pstore = [&](int pass, int v) {
                const int ntile = (pass ? 3 : 4) * NQ4;
                const int u0 = 2 * v, u1 = (2 * v + 1 < ntile) ? 2 * v + 1 : 2 * v;
                const f32x4 t0 = acc4[pass][u0 / NQ4][u0 % NQ4], t1 = acc4[pass][u1 / NQ4][u1 % NQ4];
                u32x4 o = {pk2(t0[0], t0[1]), pk2(t0[2], t0[3]), pk2(t1[0], t1[1]), pk2(t1[2], t1[3])};
                __builtin_amdgcn_raw_buffer_store_b128(o, rsP, pass ? pbase2 : pbase1, v * 1024, 16);
                SBAR(); asm volatile("s_nop 1"); SBAR();
            };
            auto ld_head = [&](int t, int u) {
                const int NT = t < 8 ? 4 : 3;
                if (u == 0) { zo[t & 1] = zr + 16 * (((t & 7) + rot4) & 7); b[0] = ld128(smem + zo[t & 1]); }
                else if (u <= 2 * NT) ah[t & 1][(u - 1) >> 1][(u - 1) & 1] = ldtr1(smem + wslot_off(t) + wo4[(u - 1) >> 1] + ((u - 1) & 1) * 2048);
                else if (u == 2 * NT + 1) b[1] = ld128(smem + zo[t & 1] + ZT);
            };
            if (GRP == 0) WAITV(outstanding_after(GRP, 17, 20));
            BARRIER();
#pragma unroll
            for (int u = 0; u < 10; ++u) ld_head(0, u);
            SBAR();
            sfor<0, 16>([&](auto t_) { constexpr int t = decltype(t_)::value;
                constexpr int NT = t < 8 ? 4 : 3, pass = t >> 3, m = t >> 1;
#pragma unroll
                for (int i = 0; i < 2 * NT; ++i) {
                    const int j = i / NT, nt = i % NT;
                    mfma((t & 7) == 0, acc4[pass][nt][j], cat(ah[t & 1][nt][0], ah[t & 1][nt][1]), b[j]);
                    if (i < NQ4 - 2) b[2 + i] = ld128(smem + zo[t & 1] + (2 + i) * ZT);
                    if ((t & 1) == 0 && m >= 1 && i >= 2) {
                        seq_issue1(16 + 2 * m + 3 + ((i - 2) >> 2), (i - 2) & 3);
                    }
                    if (t >= 8 && i == 1 && 2 * (t - 8) < NB1) pstore(0, 2 * (t - 8));
                    SBAR();
                }
                if (t + 1 < 16 && (t & 1)) {
                    constexpr int m1 = (t + 1) >> 1;
                    if (GRP == 0) WAITV(x_wait4(m1));
                    BARRIER();
                }
                if (t == 7) { wo4_init(1); SBAR(); }
                constexpr int NU1 = (NQ4 - 2) * NT;
                constexpr int NH1 = (t + 1 < 8 ? 10 : 8);
#pragma unroll
                for (int i = 0; i < NU1; ++i) {
                    const int j = 2 + i / NT, nt = i % NT;
                    mfma((t & 7) == 0, acc4[pass][nt][j], cat(ah[t & 1][nt][0], ah[t & 1][nt][1]), b[j]);
                    if (t + 1 < 16) {
                        constexpr int per = (NH1 + NU1 - 1) / NU1;
#pragma unroll
                        for (int e = 0; e < per; ++e) if (i * per + e < NH1) ld_head(t + 1, i * per + e);
                    }
                    if ((t & 1) == 0 && m >= 1 && 2 * NT - 2 + i < 8) {
                        const int pc = 2 * NT - 2 + i;
                        seq_issue1(16 + 2 * m + 3 + (pc >> 2), pc & 3);
                    }
                    if (t >= 8 && i == 1 && 2 * (t - 8) + 1 < NB1) pstore(0, 2 * (t - 8) + 1);
                    SBAR();
                }
            });
            ACCWAIT();
#pragma unroll
            for (int v = 0; v < NB2; ++v) pstore(1, v);
        }
    }
}

constexpr size_t FLAG_OFF = 48u * 1024 * 1024;
constexpr size_t EPOCH_OFF = FLAG_OFF + 256 * 256;

__global__ __launch_bounds__(512, 2) void mha_main(const short* __restrict__ x, const short* __restrict__ kc,
                                                   const short* __restrict__ vc, const short* __restrict__ wqp,
                                                   const short* __restrict__ wop, char* __restrict__ part,
                                                   unsigned short* __restrict__ out) {
    __shared__ __attribute__((aligned(16))) char smem_[LDS_TOTAL];
    lptr smem = (lptr)smem_;
    const int bid = blockIdx.x;
    const int xcd = bid & 7, r = bid >> 3;
    const int b = (xcd & 1) * 8 + (r & 7), h = (xcd >> 1) * 4 + (r >> 3);
    const int bh = b * NH + h;
#define ROT1 (((r & 7) + 2 * (r >> 3)) % 7)
    const int rot1 = ROT1, rot4 = r & 7;
    const int tid = threadIdx.x, lane = tid & 63;
    const int w = __builtin_amdgcn_readfirstlane(tid >> 6);
    gu64* epoch = (gu64*)(part + EPOCH_OFF + (size_t)b * 256);
    if (w == 0) {
        const unsigned long long e = __hip_atomic_load(epoch, __ATOMIC_RELAXED, __HIP_MEMORY_SCOPE_AGENT) + 1ull;
        if (lane == 0) *(AS3 unsigned long long*)(smem + TAGA) = e;
    }
    gu64* flag1 = (gu64*)(part + FLAG_OFF + (size_t)bh * 256);
    __amdgpu_buffer_rsrc_t rsX = __builtin_amdgcn_make_buffer_rsrc((void*)(x + (size_t)b * SQ * DM), 0, SQ * DM * 2, 0x00020000);
    __amdgpu_buffer_rsrc_t rsWq = __builtin_amdgcn_make_buffer_rsrc((void*)(wqp + h * DH), 0, (DM * NH * DH - h * DH) * 2, 0x00020000);
    __amdgpu_buffer_rsrc_t rsK = __builtin_amdgcn_make_buffer_rsrc((void*)(kc + (size_t)bh * SK * DH), 0, SK * DH * 2, 0x00020000);
    __amdgpu_buffer_rsrc_t rsV = __builtin_amdgcn_make_buffer_rsrc((void*)(vc + (size_t)bh * SK * DH), 0, SK * DH * 2, 0x00020000);
    __amdgpu_buffer_rsrc_t rsWo = __builtin_amdgcn_make_buffer_rsrc((void*)(wop + (size_t)h * DH * DM), 0, DH * DM * 2, 0x00020000);
    __amdgpu_buffer_rsrc_t rsP = __builtin_amdgcn_make_buffer_rsrc((void*)(part + (size_t)bh * PART_WG), 0, PART_WG, 0x00020000);
    if (w < 4) wave_main<0>(smem, w, lane, rot1, rot4, flag1, rsX, rsWq, rsK, rsV, rsWo, rsP);
    else wave_main<1>(smem, w - 4, lane, rot1, rot4, flag1, rsX, rsWq, rsK, rsV, rsWo, rsP);

    int tid2 = w * 64 + fresh_lane();
#define tid tid2
    const unsigned long long tag = *(AS3 const unsigned long long*)(smem + TAGA);
    const int p2 = tid >= 288 ? 1 : 0;
    gu64* fl = (gu64*)(part + FLAG_OFF + (size_t)(b * NH + (tid & 15)) * 256);
    asm volatile("s_waitcnt vmcnt(0)\n\ts_barrier" ::: "memory");
    if (tid == 448) __hip_atomic_store(flag1, tag, __ATOMIC_RELAXED, __HIP_MEMORY_SCOPE_AGENT);
    {
        __amdgpu_buffer_rsrc_t rsB = __builtin_amdgcn_make_buffer_rsrc((void*)(part + (size_t)b * NH * PART_WG), 0, NH * PART_WG, 0x00020000);
        const int gi = p2 ? 4608 + h * 224 + (tid - 288) : h * 288 + tid;
        u32x4 v[16];
#pragma unroll
        for (int hh = 0; hh < 16; ++hh) v[hh] = u32x4{0, 0, 0, 0};
        unsigned have = 0;
        const unsigned rdya = (unsigned)(size_t)(smem + RDYA);
        for (unsigned spin = 0; spin < (1u << 17) && have != 0xffffu; ++spin) {
            unsigned rdy;
            if (w == PW) {
                const unsigned long long fv = __hip_atomic_load(fl, __ATOMIC_RELAXED, __HIP_MEMORY_SCOPE_AGENT);
                rdy = (unsigned)__ballot(fv == tag) & 0xffffu;
                asm volatile("ds_write_b32 %0, %1" ::"v"(rdya), "v"(rdy) : "memory");
            } else {
                unsigned rv;
                asm volatile("ds_read_b32 %0, %1\n\ts_waitcnt lgkmcnt(0)" : "=v"(rv) : "v"(rdya) : "memory");
                rdy = __builtin_amdgcn_readfirstlane(rv);
            }
            const unsigned fresh = rdy & ~have;
            asm volatile("" ::: "memory");
#pragma unroll
            for (int hh = 0; hh < 16; ++hh)
                if (fresh >> hh & 1) v[hh] = __builtin_amdgcn_raw_buffer_load_b128(rsB, gi * 16, hh * PART_WG, 16);
            have |= fresh;
            if (have != 0xffffu && fresh == 0) __builtin_amdgcn_s_sleep(PSLEEP);
        }
        float sacc[8];
#pragma unroll
        for (int e = 0; e < 8; ++e) sacc[e] = 0.f;
#pragma unroll
        for (int hh = 0; hh < 16; ++hh)
#pragma unroll
            for (int e = 0; e < 4; ++e) {
                sacc[2 * e] += __builtin_bit_cast(float, v[hh][e] << 16);
                sacc[2 * e + 1] += __builtin_bit_cast(float, v[hh][e] & 0xffff0000u);
            }
        const int gl = gi & 63, blk = gi >> 6;
        int grp, wq, v_;
        if (blk < 32) { grp = 0; wq = blk >> 3; v_ = blk & 7; }
        else if (blk < 72) { grp = 1; wq = (blk - 32) / 10; v_ = (blk - 32) % 10; }
        else if (blk < 96) { grp = 0; wq = (blk - 72) / 6; v_ = (blk - 72) % 6; }
        else { grp = 1; wq = (blk - 96) >> 3; v_ = (blk - 96) & 7; }
        const int NQ = grp == 0 ? 4 : 5, Q0 = grp == 0 ? 0 : 4, NT = p2 ? 3 : 4;
#pragma unroll
        for (int t = 0; t < 2; ++t) {
            const int u = 2 * v_ + t;
            if (u < NT * NQ) {
                const int nt = u / NQ, j = u % NQ;
                const int q = (Q0 + j) * 16 + (gl & 15), n = p2 * 256 + 16 * (NT * wq + nt) + 4 * (gl >> 4);
                u32x2 o = {pk2(sacc[4 * t], sacc[4 * t + 1]), pk2(sacc[4 * t + 2], sacc[4 * t + 3])};
                *(u32x2*)(out + ((size_t)(b * SQ + q) * DM + n)) = o;
            }
        }
    }
    if (h == 0 && tid == 448) __hip_atomic_store(epoch, tag, __ATOMIC_RELAXED, __HIP_MEMORY_SCOPE_AGENT);
}

extern "C" void kernel_launch(void* const* d_in, const int* in_sizes, int n_in,
                              void* d_out, int out_size, void* d_ws, size_t ws_size,
                              hipStream_t stream) {
    mha_main<<<256, 512, 0, stream>>>((const short*)d_in[0], (const short*)d_in[1], (const short*)d_in[2],
                                      (const short*)d_in[3], (const short*)d_in[4], (char*)d_ws, (unsigned short*)d_out);
}
```
